```python
import torch

def _softmax(x, axis=-1):
    return torch.softmax(x, dim=axis)
def _gelu(x):
    return torch.nn.functional.gelu(x, approximate="tanh")
def _silu(x):
    return torch.nn.functional.silu(x)
def _sigmoid(x):
    return torch.sigmoid(x)
def _layernorm(x, axis=-1, eps=1e-5):
    m = x.mean(dim=axis, keepdim=True)
    v = x.var(dim=axis, keepdim=True, unbiased=False)
    return (x - m) / torch.sqrt(v + eps)
def _scatter_add(tgt, idx, src, axis=0):
    return tgt.clone().index_add_(axis, idx, src)
def _segment_sum(src, seg, n):
    out = torch.zeros((n,) + src.shape[1:], dtype=src.dtype)
    return out.index_add_(0, seg, src)
def _pad(x, pw):
    flat = tuple(v for pair in reversed(pw) for v in pair)
    return torch.nn.functional.pad(x, flat)

kernel_name = 'recurrent_scan__spectral_gfnet__dense_activation_gelu__prompt'

def setup_inputs():

    _w = (lambda a: (lambda t: t.to(torch.bfloat16) if t.is_floating_point() else t)(torch.as_tensor(a)))
    def _A_setup():
        B, S, D = 1, 2048, 640
        Df = D // 2 + 1
        return {
            "x": _w((lambda *s: torch.randn(*s, dtype=torch.bfloat16) * 0.05)(B, S, D)),
            "w_r": _w((lambda *s: torch.randn(*s, dtype=torch.bfloat16) * 3.2)(S, Df) * 0.02),
            "w_i": _w((lambda *s: torch.randn(*s, dtype=torch.bfloat16) * 3.2)(S, Df) * 0.02),
        }
    _A_in = _A_setup()

    def _B_setup():
        B, S, D, F = 1, 2048, 640, 1728
        return {
            "x": _w((lambda *s: torch.randn(*s, dtype=torch.bfloat16) * 0.05)(B, S, D)),
            "w1": _w((lambda *s: torch.randn(*s, dtype=torch.bfloat16) * 3.2)(D, F) * (D ** -0.5)),
            "b1": _w((lambda *s: torch.randn(*s, dtype=torch.bfloat16) * 3.2)(F) * 0.01),
            "w2": _w((lambda *s: torch.randn(*s, dtype=torch.bfloat16) * 0.4)(F, D) * (F ** -0.5)),
            "b2": _w((lambda *s: torch.randn(*s, dtype=torch.bfloat16) * 0.4)(D) * 0.01),
        }
    _B_in = _B_setup()

    return {
        "A_x": _A_in["x"],
        "A_w_r": _A_in["w_r"],
        "A_w_i": _A_in["w_i"],
        "B_x": _B_in["x"],
        "B_w1": _B_in["w1"],
        "B_b1": _B_in["b1"],
        "B_w2": _B_in["w2"],
        "B_b2": _B_in["b2"],
    }

def reference(A_x, A_w_r, A_w_i, B_x, B_w1, B_b1, B_w2, B_b2):
    def _A_ref(x, w_r, w_i):
        B, S, D = x.shape
        X = (lambda x, axes=(-2,-1): torch.fft.rfft2(x.float(), dim=axes))(x, axes=(-2, -1))
        W = w_r + 1j * w_i
        X = X * W
        y = (lambda x, s, axes=(-2,-1): torch.fft.irfft2(x, s=s, dim=axes).to(torch.bfloat16))(X, (S, D), axes=(-2, -1))
        return y

    def _B_ref(x, w1, b1, w2, b2):
        h = torch.einsum("bsd,df->bsf", x, w1) + b1
        h = _gelu(h)
        return torch.einsum("bsf,fd->bsd", h, w2) + b2

    x = A_x
    xn = _layernorm(x, axis=-1)
    s = _A_ref(xn, A_w_r, A_w_i)
    g = _silu(_B_ref(xn, B_w1, B_b1, B_w2, B_b2))
    return x + s * g
```

```cpp
#include <hip/hip_runtime.h>
typedef unsigned short u16;
typedef unsigned u32;
typedef unsigned long long u64;
typedef __attribute__((ext_vector_type(8))) __bf16 bf16x8;
typedef __attribute__((ext_vector_type(2))) __bf16 bf16x2;
typedef __attribute__((ext_vector_type(4))) short s16x4;
typedef __attribute__((ext_vector_type(2))) float f32x2;
typedef __attribute__((ext_vector_type(4))) float f32x4;
typedef __attribute__((ext_vector_type(16))) float f32x16;
typedef __attribute__((ext_vector_type(2))) unsigned u32x2;
typedef __attribute__((ext_vector_type(4))) unsigned u32x4;
#define LAS __attribute__((address_space(3)))
#define DI __device__ __forceinline__

#define PADB 0
constexpr u32 SX = 4096 + PADB, SW = 8192 + PADB;
constexpr u32 OFF_H   = 0;
constexpr u32 OFF_X1  = OFF_H + 32 * 54 * 4096;
constexpr u32 OFF_WT  = OFF_X1 + 640 * SX;
constexpr u32 OFF_BT  = OFF_WT + 321 * SW;
constexpr u32 OFF_ZT  = OFF_BT + 8 * 54 * 5120;
constexpr u32 OFF_GP  = OFF_ZT + 640 * SX;
constexpr u32 OFF_CNT = OFF_GP + 256 * 20480;

constexpr double kPI = 3.14159265358979323846;
constexpr double tcos(double x) { double x2 = x * x, t = 1, s = 1; for (int i = 1; i <= 10; i++) { t *= -x2 / ((2 * i - 1) * (2 * i)); s += t; } return s; }
constexpr double tsin(double x) { double x2 = x * x, t = x, s = x; for (int i = 1; i <= 10; i++) { t *= -x2 / ((2 * i) * (2 * i + 1)); s += t; } return s; }
struct CosTab { float v[320]; };
constexpr CosTab make_cos() {
  CosTab c{};
  for (int i = 0; i < 320; i++) {
    int j = i; bool neg = false;
    if (j > 160) j = 320 - j;
    if (j > 80) { j = 160 - j; neg = true; }
    double v = j <= 40 ? tcos(2 * kPI * j / 320) : tsin(2 * kPI * (80 - j) / 320);
    c.v[i] = (float)(neg ? -v : v);
  }
  return c;
}
constexpr CosTab kCos = make_cos();
constexpr float wre(int n, int N) { return kCos.v[(n % N) * (320 / N)]; }
constexpr float wim(int n, int N) { return kCos.v[((n % N) * (320 / N) + 80) % 320]; }
constexpr u16 cbf(float f) { u32 u = __builtin_bit_cast(u32, f); u += 0x7fffu + ((u >> 16) & 1u); return (u16)(u >> 16); }
struct alignas(16) TabA { u16 f32[4][64][8]; u16 g20[6][64][8]; };
struct alignas(16) TabM1 { u16 v[8][64][8]; };
struct alignas(16) TabF32 { u16 v[4][64][8]; };
struct alignas(16) TabF64 { u16 v[16][64][8]; };
constexpr TabA make_tabA() {
  TabA t{};
  for (int tt = 0; tt < 2; tt++) for (int l = 0; l < 64; l++) for (int j = 0; j < 8; j++) {
    int g = l >> 4, k1 = 16 * tt + (l & 15), n1 = 16 * (g >> 1) + 8 * (j >> 2) + 4 * (g & 1) + (j & 3);
    t.f32[tt * 2 + 0][l][j] = cbf(wre(n1 * k1, 32));
    t.f32[tt * 2 + 1][l][j] = cbf(wim(n1 * k1, 32));
  }
  for (int T = 0; T < 3; T++) for (int ks = 0; ks < 2; ks++) for (int l = 0; l < 64; l++) for (int j = 0; j < 8; j++) {
    int row = 16 * T + (l & 15), g = l >> 4, k2 = row >> 1, oc = row & 1, ic = j >> 2;
    int n2 = ks == 0 ? 4 * g + (j & 3) : 16 + (j & 3);
    float gr = wre(n2 * k2, 20), gi = wim(n2 * k2, 20);
    float v = oc == 0 ? (ic == 0 ? gr : -gi) : (ic == 0 ? gi : gr);
    t.g20[T * 2 + ks][l][j] = row < 40 ? cbf(v) : (u16)0;
  }
  return t;
}
constexpr TabM1 make_m1() {
  TabM1 t{};
  for (int ks = 0; ks < 2; ks++) for (int tt = 0; tt < 2; tt++) for (int l = 0; l < 64; l++) for (int j = 0; j < 8; j++) {
    int k1 = 16 * tt + (l & 15), q = 4 * (l >> 4) + (j >> 1), ic = j & 1;
    float far = wre(2 * q * k1, 32), fai = wim(2 * q * k1, 32), fbr = wre((2 * q + 1) * k1, 32), fbi = wim((2 * q + 1) * k1, 32);
    float vr = ks == 0 ? (ic == 0 ? far + fbi : fbr - fai) : (ic == 0 ? far - fbi : fai + fbr);
    float vi = ks == 0 ? (ic == 0 ? fai - fbr : far + fbi) : (ic == 0 ? fai + fbr : fbi - far);
    t.v[(ks * 2 + tt) * 2 + 0][l][j] = cbf(vr);
    t.v[(ks * 2 + tt) * 2 + 1][l][j] = cbf(vi);
  }
  return t;
}
constexpr TabF32 make_f32() {
  TabF32 t{};
  for (int u = 0; u < 2; u++) for (int tt = 0; tt < 2; tt++) for (int l = 0; l < 64; l++) for (int j = 0; j < 4; j++) {
    int n1 = 16 * u + (l & 15), k1 = 16 * tt + 4 * (l >> 4) + j;
    t.v[u * 2 + tt][l][j] = cbf(wre(n1 * k1, 32));
    t.v[u * 2 + tt][l][j + 4] = cbf(wim(n1 * k1, 32));
  }
  return t;
}
constexpr TabF64 make_f64() {
  TabF64 t{};
  for (int w = 0; w < 4; w++) for (int wp = 0; wp < 4; wp++) for (int l = 0; l < 64; l++) for (int j = 0; j < 4; j++) {
    int a = 16 * w + (l & 15), b = 16 * wp + 4 * (l >> 4) + j;
    t.v[w * 4 + wp][l][j] = cbf(wre(a * b, 64));
    t.v[w * 4 + wp][l][j + 4] = cbf(wim(a * b, 64));
  }
  return t;
}
__device__ constexpr TabA dTabA = make_tabA();
__device__ constexpr TabM1 dTabM1 = make_m1();
__device__ constexpr TabF32 dTabF32 = make_f32();
__device__ constexpr TabF64 dTabF64 = make_f64();

DI void glds16(const void* sbase, u32 voff, u32 ldsaddr) {
  u32 keep;
  asm volatile("s_mov_b32 %0, m0\n\ts_mov_b32 m0, %3\n\ts_nop 0\n\tglobal_load_lds_dwordx4 %1, %2\n\ts_mov_b32 m0, %0"
               : "=&s"(keep) : "v"(voff), "s"(sbase), "s"(ldsaddr) : "memory");
}
DI void glds4(const void* sbase, u32 voff, u32 ldsaddr) {
  u32 keep;
  asm volatile("s_mov_b32 %0, m0\n\ts_mov_b32 m0, %3\n\ts_nop 0\n\tglobal_load_lds_dword %1, %2\n\ts_mov_b32 m0, %0"
               : "=&s"(keep) : "v"(voff), "s"(sbase), "s"(ldsaddr) : "memory");
}
#define GL(n, off) "global_load_lds_dwordx4 %" #n ", %0 offset:" #off "\n\t"
DI void glds8(const void* sbase, u32 m0v, const u32* v) {
  asm volatile("s_mov_b32 m0, %1\n\ts_nop 0\n\t" GL(2, -4096) GL(3, -3072) GL(4, -2048) GL(5, -1024) GL(6, 0) GL(7, 1024) GL(8, 2048) GL(9, 3072)
               :: "s"(sbase), "s"(m0v), "v"(v[0]), "v"(v[1]), "v"(v[2]), "v"(v[3]), "v"(v[4]), "v"(v[5]), "v"(v[6]), "v"(v[7]) : "memory");
}
template <int N> DI void gldsc(const void* sbase, u32 m0v, u32 v) {
  if (N == 8) asm volatile("s_mov_b32 m0, %1\n\ts_nop 0\n\t" GL(2, -4096) GL(2, -3072) GL(2, -2048) GL(2, -1024) GL(2, 0) GL(2, 1024) GL(2, 2048) GL(2, 3072) :: "s"(sbase), "s"(m0v), "v"(v) : "memory");
  else asm volatile("s_mov_b32 m0, %1\n\ts_nop 0\n\t" GL(2, -4096) GL(2, -3072) GL(2, -2048) GL(2, -1024) GL(2, 0) GL(2, 1024) GL(2, 2048) :: "s"(sbase), "s"(m0v), "v"(v) : "memory");
}
#define STAUX 16
typedef __amdgpu_buffer_rsrc_t rsrc_t;
typedef __attribute__((address_space(1))) u32 gu32;
DI void glds4c(const void* sbase, u32 m0v, u32 v) {
  asm volatile("s_mov_b32 m0, %1\n\ts_nop 0\n\t" GL(2, 0) GL(2, 1024) GL(2, 2048) GL(2, 3072) :: "s"(sbase), "s"(m0v), "v"(v) : "memory");
}
DI rsrc_t wsr(char* ws) { return __builtin_amdgcn_make_buffer_rsrc(ws, 0, 32 << 20, 0x00020000); }
DI void st16(rsrc_t r, u32 off, u32x4 v) { __builtin_amdgcn_raw_buffer_store_b128(v, r, off, 0, STAUX); }
DI float sum32(float v) {
  float a = v, b = v;
  asm volatile("s_nop 1\n\tv_permlane32_swap_b32 %0, %1\n\ts_nop 1" : "+v"(a), "+v"(b));
  return a + b;
}
DI u32 rfl(u32 v) { return __builtin_amdgcn_readfirstlane(v); }
DI u32 ldsv(LAS char* p) { return *(volatile LAS u32*)p; }
DI u32x2 ldsv2(LAS char* p) { return *(volatile LAS u32x2*)p; }
DI bool allge(u32 v, u32 t) { return (((v | 0x80808080u) - t * 0x01010101u) & 0x80808080u) == 0x80808080u; }
#define LA 5
#define LDAUX 0
#define ZJ 4
#define PSL 8
#define GO 0
#define AW 8
#define SIDE0 6
#define ZSTART 5
#define SPIN(notready, reload) do { if (notready) { u32 sp_ = 0; do { reload; } while ((notready) && ++sp_ < (1u << 16)); } } while (0)
template <int N> DI void waitv() { asm volatile("s_waitcnt vmcnt(%0)" ::"n"(N) : "memory"); }
DI void waitl() { asm volatile("s_waitcnt lgkmcnt(0)" ::: "memory"); }
DI void bar() { __builtin_amdgcn_s_barrier(); }
DI u32 pk(float a, float b) { f32x2 f = {a, b}; return __builtin_bit_cast(u32, __builtin_convertvector(f, bf16x2)); }
DI float rbf(float a) { return __builtin_bit_cast(float, pk(a, a) << 16); }
DI float blo(u32 v) { return __builtin_bit_cast(float, v << 16); }
DI float bhi(u32 v) { return __builtin_bit_cast(float, v & 0xffff0000u); }
DI float dot2(u32 a, u32 b, float c) { return __builtin_amdgcn_fdot2_f32_bf16(__builtin_bit_cast(bf16x2, a), __builtin_bit_cast(bf16x2, b), c, false); }
DI f32x4 mfma16(u32x4 a, u32x4 b, f32x4 c) { return __builtin_amdgcn_mfma_f32_16x16x32_bf16(__builtin_bit_cast(bf16x8, a), __builtin_bit_cast(bf16x8, b), c, 0, 0, 0); }
DI f32x16 mfma32(u32x4 a, u32x4 b, f32x16 c) { return __builtin_amdgcn_mfma_f32_32x32x16_bf16(__builtin_bit_cast(bf16x8, a), __builtin_bit_cast(bf16x8, b), c, 0, 0, 0); }
DI u32x2 trd(LAS char* p) { return __builtin_bit_cast(u32x2, __builtin_amdgcn_ds_read_tr16_b64_v4i16((LAS s16x4*)p)); }
DI u32x4 trd2(LAS char* p0, LAS char* p1) { u32x2 a = trd(p0), b = trd(p1); return (u32x4){a.x, a.y, b.x, b.y}; }
DI u32x4 ldsr(LAS char* p) { return *(LAS u32x4*)p; }
DI float rcp(float x) { return __builtin_amdgcn_rcpf(x); }
DI void lnstats(float sum, float sq, float& mb, float& rs) {
  float m = sum * (1.0f / 640), var = fmaxf(sq * (1.0f / 640) - m * m, 0.0f);
  mb = rbf(m);
  rs = rcp(rbf(__builtin_amdgcn_sqrtf(rbf(rbf(var) + 1e-5f))));
}
constexpr u32 ONES = 0x3f803f80u;

#define STAMP(kk,i)
#define TRDECL
#define TR(i)
#define TREND
#define STAMPW(kk,i)

constexpr u32 K1_P1 = 131072, K1_P2 = K1_P1 + 14336, K1_MISC = K1_P2 + 10240, K1_FL = K1_MISC + 5376, K1_DUM = K1_FL + 16, K1_LDS = K1_DUM + 256;

DI void g1_compute(const u16* x, const u16* w1, char* ws, LAS char* sm, int tid, int l, int wv, int w, int g, int ct, int gi, int ci7, int rot0) {
  STAMP(0,0);
  const rsrc_t wr_ = wsr(ws);
  const int fq = wv >> 1, sh = wv & 1, hh = l >> 5, rot = fq + sh;
  u32 aoff[4][2], boff[4];
  {
    int blk = (l >> 4) & 1, q = (l & 15) >> 2, p = l & 3, row = 64 * sh + (l & 31);
#pragma unroll
    for (int t = 0; t < 4; t++) {
#pragma unroll
      for (int u = 0; u < 2; u++) {
        int r = 8 * hh + 4 * u + q, ch = 4 * fq + 2 * blk + (p >> 1);
        aoff[t][u] = ((t + rot) & 3) * 4096 + 256 * r + 16 * (ch ^ (((r & 3) << 2) | ((r >> 2) & 3))) + 8 * (p & 1);
      }
      boff[t] = 65536 + row * 128 + (((2 * ((t + rot) & 3) + hh) ^ ((row >> 1) & 7)) * 16);
    }
  }
#pragma unroll
  for (int t = 0; t < 4; t++) asm volatile("" : "+v"(aoff[t][0]), "+v"(aoff[t][1]), "+v"(boff[t]));
  f32x16 acc0 = {}, acc1 = {};
  float s0 = 0, q0 = 0, s1 = 0, q1 = 0, cs = 0, cs2 = 0;
  STAMP(0,1);
  u32x4 a[2][4], b0[2][4], b1[2][4];
#define SB __builtin_amdgcn_sched_barrier(0)
  auto step = [&](int ch, bool rd, bool mm) {
    const int c = ch & 1, n = c ^ 1, so = ((ch + 1) & 3) * 16384;
    if (rd) bar();
    SB;
#pragma unroll
    for (int t = 0; t < 4; t++) {
      if (mm) acc0 = mfma32(a[c][t], b0[c][t], acc0);
      SB;
      if (rd) a[n][t] = trd2(sm + aoff[t][0] + so, sm + aoff[t][1] + so);
      if (mm) { float lo = blo(b0[c][0][t]), hi = bhi(b0[c][0][t]); s0 += lo; q0 = fmaf(lo, lo, q0); s0 += hi; q0 = fmaf(hi, hi, q0); }
      SB;
      if (mm) acc1 = mfma32(a[c][t], b1[c][t], acc1);
      SB;
      if (rd) { b0[n][t] = ldsr(sm + boff[t] + so); b1[n][t] = ldsr(sm + boff[t] + so + 4096); }
      if (mm) { float lo = blo(b1[c][0][t]), hi = bhi(b1[c][0][t]); s1 += lo; q1 = fmaf(lo, lo, q1); s1 += hi; q1 = fmaf(hi, hi, q1); }
      SB;
    }
    if (mm) {
#pragma unroll
      for (int e = 0; e < 4; e++) { cs = dot2(a[c][0][e], ONES, cs); cs2 = dot2(a[c][2][e], ONES, cs2); }
    }
    asm volatile("" : "+v"(s0), "+v"(q0), "+v"(s1), "+v"(q1), "+v"(cs), "+v"(cs2), "+v"(acc0), "+v"(acc1));
    SB;
  };
  step(-1, true, false);
  STAMP(0,2);
#pragma unroll 1
  for (int it = 0; it < 2; it++) { step(0, true, true); step(1, true, true); step(2, true, true); step(3, true, true); }
  step(0, true, true);
  step(1, false, true);
  cs += cs2;
  STAMP(0,3);
  s0 = sum32(s0); q0 = sum32(q0); s1 = sum32(s1); q1 = sum32(q1); cs = sum32(cs);
  LAS float* st = (LAS float*)(sm + K1_MISC + 256);
  LAS float* csl = (LAS float*)(sm + K1_MISC + 256 + 4096);
  *(LAS f32x2*)(st + (fq * 128 + 64 * sh + l) * 2) = hh ? (f32x2){s1, q1} : (f32x2){s0, q0};
  if (hh == 0) csl[sh * 128 + 32 * fq + l] = cs;
  waitl(); bar();
  if (ct == 13 && fq >= 2) return;
  float mr[2], rs[2];
#pragma unroll
  for (int b = 0; b < 2; b++) {
    f32x2 a = {0, 0};
#pragma unroll
    for (int f = 0; f < 4; f++) a += *(LAS f32x2*)(st + (f * 128 + 64 * sh + 32 * b + (l & 31)) * 2);
    float mb; lnstats(a.x, a.y, mb, rs[b]); mr[b] = -mb * rs[b];
  }
  const u32 hp = OFF_H + (((8 * w + 2 * g + sh) * 4 + ((l >> 4) & 1)) * 54 + 4 * ct + fq) * 1024 + (hh * 16 + (l & 15)) * 16;
#pragma unroll
  for (int sg = 0; sg < 2; sg++) {
    f32x2 cf[4], bf[4];
#pragma unroll
    for (int u = 0; u < 2; u++) {
      int f = 32 * fq + 16 * sg + 8 * u + 4 * hh;
      f32x4 c = *(LAS f32x4*)(csl + f) + *(LAS f32x4*)(csl + 128 + f);
      u32x2 bb = *(LAS u32x2*)(sm + K1_MISC + f * 2);
      cf[2 * u] = (f32x2){c.x, c.y}; cf[2 * u + 1] = (f32x2){c.z, c.w};
      bf[2 * u] = (f32x2){blo(bb.x), bhi(bb.x)}; bf[2 * u + 1] = (f32x2){blo(bb.y), bhi(bb.y)};
    }
#pragma unroll
    for (int b = 0; b < 2; b++) {
      u32x4 o;
#pragma unroll
      for (int j = 0; j < 4; j++) {
        f32x2 a = b ? (f32x2){acc1[8 * sg + 2 * j], acc1[8 * sg + 2 * j + 1]} : (f32x2){acc0[8 * sg + 2 * j], acc0[8 * sg + 2 * j + 1]};
        f32x2 v = a * rs[b] + (cf[j] * mr[b] + bf[j]);
        f32x2 z = v * (v * v * -0.10294324f + -2.3022082f);
        f32x2 d = (f32x2){__builtin_amdgcn_exp2f(z.x), __builtin_amdgcn_exp2f(z.y)} + 1.0f;
        v *= (f32x2){rcp(d.x), rcp(d.y)};
        o[j] = pk(v.x, v.y);
      }
      st16(wr_, hp + b * (2 * 54 * 1024) + sg * 512, o);
    }
  }
  STAMP(0,4);
}

DI void g1_loader(const u16* x, const u16* w1, const u16* b1, LAS char* sm, int l, int lw, int w, int g, int ct, int rot0) {
  const u32 sb0 = (u32)(size_t)sm;
  u32 vo[8];
  const char *base; u32 step, grp;
  if (lw == 0) {
    const int q = l >> 4;
#pragma unroll
    for (int j = 0; j < 8; j++) {
      int ch = (l & 15) ^ ((q << 2) | (j & 3));
      if (ct == 13) ch &= 7;
      vo[j] = (4 * j + q) * 3456 + ch * 16 + 4096 - 1024 * j;
    }
    base = (const char*)w1 + ct * 256; step = 64 * 3456; grp = 32 * 3456;
  } else {
    const int rr = l >> 3;
#pragma unroll
    for (int j = 0; j < 8; j++)
      vo[j] = (64 * (j + 16 * (rr >> 2)) + 16 * w + 4 * g + (rr & 3)) * 1280 + (((l & 7) ^ (4 * (j & 1) + (rr >> 1))) & 7) * 16 + 4096 - 1024 * j;
    base = (const char*)x; step = 128; grp = 8 * 64 * 1280;
  }
  int kc = rot0;
  auto issue = [&](int i) {
    const char* p = base + kc * step;
    u32 m = sb0 + (i & 3) * 16384 + lw * 65536 + 4096;
    glds8(p, m, vo); glds8(p + grp, m + 8192, vo);
    kc = kc == 9 ? 0 : kc + 1;
  };
  if (lw == 0) glds4(b1, (u32)(ct * 256 + ((ct == 13) ? (l & 31) : l) * 4), sb0 + K1_MISC);
  issue(0); issue(1);
#pragma unroll 1
  for (int i = 0; i < 8; i++) {
    waitv<16>(); bar();
    issue(i + 2);
  }
  waitv<16>(); bar();
  waitv<0>(); bar();
  waitl(); bar();
}

DI void g1_side(const u16* wr, const u16* wi, const u16* w2, char* ws, LAS char* sm, int l, int sw, int gi) {
  const u32 sb0 = (u32)(size_t)sm;
  const rsrc_t wr_ = wsr(ws);
  const int ru = gi & 63, cc = gi >> 6, cb = cc == 2 ? 209 : 107 * cc, kh = gi >> 1, p2h = gi & 1;
  {
    const char *c0, *c1;
    asm volatile("s_getpc_b64 s[40:41]\n\ts_add_u32 s40, s40, k1@rel32@lo+4\n\ts_addc_u32 s41, s41, k1@rel32@hi+12\n\ts_mov_b64 %0, s[40:41]\n\t"
                 "s_getpc_b64 s[40:41]\n\ts_add_u32 s40, s40, kend@rel32@lo+4\n\ts_addc_u32 s41, s41, kend@rel32@hi+12\n\ts_mov_b64 %1, s[40:41]"
                 : "=&s"(c0), "=&s"(c1) :: "scc", "s40", "s41");
    const int pc = (gi % 28) * 2 + sw;
    const char* p = pc < 6 ? c0 + pc * 8192 : pc < 8 ? (const char*)&dTabF64 + (pc - 6) * 8192 : pc == 8 ? (const char*)&dTabM1 : (const char*)&dTabF32;
    const char* e = pc < 6 ? c1 : pc == 9 ? p + 4096 : p + 8192;
    p += l * 128;
    if (pc < 10 && p < e) asm volatile("s_mov_b32 m0, %1\n\ts_nop 0\n\tglobal_load_lds_dword %0, off" :: "v"(p), "s"(K1_DUM) : "memory");
  }
  u32x4 vr[4], vi[4];
  u32 o1[4], o2[5], pl[4];
  const int k0 = 16 * ((ru >> 3) & 1) + 8 * (ru & 1) + 32 * (16 * (ru >> 4) + 4 * ((ru >> 1) & 3));
#pragma unroll
  for (int r = 0; r < 4; r++) {
    int u = min(64 * r + l, 223), rho = u / 7, cg = u % 7;
    o1[r] = gi < 192 ? (u32)((k0 + (rho & 7) + 32 * (rho >> 3)) * 321 + cb + 56 * sw + 8 * cg) * 2 : 0u;
    pl[r] = K1_P1 + sw * 7168 + cg * 1024 + (((rho & 7) ^ cg) * 16) + (rho >> 3) * 4;
  }
#pragma unroll
  for (int i = 0; i < 5; i++) { int e = 64 * i + l; o2[i] = gi < 216 ? (u32)((16 * kh + e / 20) * 1280 + p2h * 640 + sw * 320 + (e % 20) * 16) : 0u; }
  asm volatile("" : "+v"(o1[0]), "+v"(o1[1]), "+v"(o1[2]), "+v"(o1[3]), "+v"(pl[0]), "+v"(pl[1]), "+v"(pl[2]), "+v"(pl[3]), "+v"(o2[0]), "+v"(o2[1]), "+v"(o2[2]), "+v"(o2[3]), "+v"(o2[4]));
  if (gi < 5 && sw == 0 && gi * 64 + l < 272) __builtin_amdgcn_raw_buffer_store_b32(0u, wr_, OFF_CNT + (gi * 64 + l) * 128, 0, 16);
#pragma unroll 1
  for (int i = 0; i < SIDE0; i++) bar();
#pragma unroll
  for (int r = 0; r < 4; r++) {
    asm volatile("global_load_dwordx4 %0, %1, %2" : "=v"(vr[r]) : "v"(o1[r]), "s"(wr) : "memory");
    asm volatile("global_load_dwordx4 %0, %1, %2" : "=v"(vi[r]) : "v"(o1[r]), "s"(wi) : "memory");
    if (r & 1) bar();
  }
#pragma unroll
  for (int i = 0; i < 5; i++) glds16(w2, o2[i], sb0 + K1_P2 + sw * 5120 + i * 1024);
#pragma unroll 1
  for (int i = SIDE0 + 2; i < 11; i++) bar();
  asm volatile("s_waitcnt vmcnt(0)" : "+v"(vr[0]), "+v"(vi[0]), "+v"(vr[1]), "+v"(vi[1]), "+v"(vr[2]), "+v"(vi[2]), "+v"(vr[3]), "+v"(vi[3])::"memory");
  if (gi < 216) {
    const int g1 = (l >> 4) & 1;
    LAS char* p = sm + K1_P2 + sw * 5120 + (4 * g1 + ((l & 15) >> 2)) * 320 + (l >> 5) * 32 + (l & 3) * 8;
    u32x4 v[5];
#pragma unroll
    for (int i = 0; i < 5; i++) v[i] = trd2(p + i * 64, p + i * 64 + 8 * 320);
#pragma unroll
    for (int i = 0; i < 5; i++) {
      int n = 2 * i + (l >> 5), c2 = 4 * p2h + 2 * sw + (n >= 5), nt = n >= 5 ? n - 5 : n;
      st16(wr_, OFF_BT + ((c2 * 54 + (kh >> 1)) * 5 + nt) * 1024 + ((2 * (kh & 1) + g1) * 16 + (l & 15)) * 16, v[i]);
    }
  }
  if (gi < 192) {
#pragma unroll
    for (int r = 0; r < 4; r++)
#pragma unroll
      for (int e = 0; e < 8; e++)
        *(LAS u32*)(sm + pl[r] + e * 128) = __builtin_amdgcn_perm(vi[r][e >> 1], vr[r][e >> 1], (e & 1) ? 0x07060302u : 0x05040100u);
    const int col = cb + 56 * sw + (l >> 3);
    const u32 wp = OFF_WT + col * SW + (((ru >> 3) * 64) + ((ru >> 1) & 3) * 16 + 8 * (ru & 1) + (l & 7)) * 16;
    u32x4 v[7];
#pragma unroll
    for (int i = 0; i < 7; i++) v[i] = ldsr(sm + K1_P1 + sw * 7168 + i * 1024 + (l >> 3) * 128 + (((l & 7) ^ i) * 16));
#pragma unroll
    for (int i = 0; i < 7; i++) st16(wr_, (u32)(col + 8 * i - 107 * cc) < 107u ? wp + i * (8 * SW) : 0xffffff00u, v[i]);
  }
}

DI void stageA(const u16* x, char* ws, LAS char* sm, int tid, int l, int wv, int xcd, int idx) {
  const u32 sb0 = (u32)(size_t)sm;
  const int w = xcd >> 1, sidx = (xcd & 1) * 4 + idx - 28, gq = sidx >> 1, o = sidx & 1;
  const int n2 = 16 * w + 8 * o + wv, c = l & 15, g = l >> 4;
  STAMP(0,0);
#pragma unroll
  for (int i = 0; i < 10; i++) {
    int e = 64 * i + l;
    glds16(x, (u32)((64 * (8 * gq + e / 80) + n2) * 1280 + (e % 80) * 16), sb0 + wv * 10240 + i * 1024);
  }
  u32x4 tf[4], tg[6];
#pragma unroll
  for (int i = 0; i < 4; i++) tf[i] = *(const u32x4*)dTabA.f32[i][l];
#pragma unroll
  for (int i = 0; i < 6; i++) tg[i] = *(const u32x4*)dTabA.g20[i][l];
  float twc[2][2][4], tws[2][2][4];
#pragma unroll
  for (int t = 0; t < 2; t++)
#pragma unroll
    for (int r = 0; r < 4; r++) {
      float a = (float)((4 * g + r) * (16 * t + c)) * (1.0f / 640), b = (float)((16 + r) * (16 * t + c)) * (1.0f / 640);
      twc[0][t][r] = __builtin_amdgcn_cosf(a); tws[0][t][r] = __builtin_amdgcn_sinf(a);
      twc[1][t][r] = __builtin_amdgcn_cosf(b); tws[1][t][r] = __builtin_amdgcn_sinf(b);
    }
#pragma unroll
  for (int t = 0; t < 2; t++)
#pragma unroll
    for (int r = 0; r < 4; r++) asm volatile("" : "+v"(twc[0][t][r]), "+v"(tws[0][t][r]), "+v"(twc[1][t][r]), "+v"(tws[1][t][r]));
  STAMP(0,1);
  waitv<0>();
  STAMP(0,2);
  LAS char* xb = sm + wv * 10240;
  float sum = 0, sq = 0;
#pragma unroll
  for (int i = 0; i < 10; i++) {
    u32x4 v = ldsr(xb + (l >> 3) * 1280 + (8 * i + (l & 7)) * 16);
#pragma unroll
    for (int e = 0; e < 4; e++) { sum = dot2(v[e], ONES, sum); sq = dot2(v[e], v[e], sq); }
  }
#pragma unroll
  for (int m = 1; m < 8; m <<= 1) { sum += __shfl_xor(sum, m); sq += __shfl_xor(sq, m); }
  float mb, rs; lnstats(sum, sq, mb, rs);
  const float dc = -32.0f * mb;
  LAS char* tb = xb + (16 * (g >> 1) + 4 * (g & 1) + ((l & 15) >> 2)) * 40;
  auto step1 = [&](u32x4 xa, u32x4 xbf, float ra, float rb, float da, float db, int mt, u32x4* outB) {
#pragma unroll
    for (int t = 0; t < 2; t++) {
      float ia = (t == 0 && c == 0) ? da : 0.0f, ib = (t == 0 && c == 0) ? db : 0.0f;
      f32x4 z = {0, 0, 0, 0};
      f32x4 p1 = mfma16(xa, tf[2 * t], (f32x4){ia, ia, ia, ia}), p3 = mfma16(xa, tf[2 * t + 1], z);
      f32x4 p4 = mfma16(xbf, tf[2 * t], (f32x4){ib, ib, ib, ib}), p2 = mfma16(xbf, tf[2 * t + 1], z);
      float re[4], im[4];
#pragma unroll
      for (int r = 0; r < 4; r++) {
        float tr = ra * p1[r] - rb * p2[r], ti = ra * p3[r] + rb * p4[r];
        re[r] = tr * twc[mt][t][r] + ti * tws[mt][t][r];
        im[r] = ti * twc[mt][t][r] - tr * tws[mt][t][r];
      }
      outB[t] = (u32x4){pk(re[0], re[1]), pk(re[2], re[3]), pk(im[0], im[1]), pk(im[2], im[3])};
    }
  };
  u32x4 tail[2];
  {
    LAS char* p = tb + (l & 3) * 2560 + 32;
    u32x4 xa = trd2(p, p + 320), xbf = trd2(p + 1280, p + 1600);
    float ra = __shfl(rs, 16 * g), rb = __shfl(rs, 16 * g + 8), da = __shfl(dc, 16 * g), db = __shfl(dc, 16 * g + 8);
    step1(xa, xbf, ra, rb, da, db, 1, tail);
  }
  u32x4 res[2][3][2];
#pragma unroll
  for (int i = 0; i < 4; i++) {
    LAS char* p = tb + i * 2560 + (l & 3) * 8;
    u32x4 xa = trd2(p, p + 320), xbf = trd2(p + 1280, p + 1600);
    float ra = __builtin_bit_cast(float, __builtin_amdgcn_readlane(__builtin_bit_cast(int, rs), 16 * i));
    float rb = __builtin_bit_cast(float, __builtin_amdgcn_readlane(__builtin_bit_cast(int, rs), 16 * i + 8));
    float da = __builtin_bit_cast(float, __builtin_amdgcn_readlane(__builtin_bit_cast(int, dc), 16 * i));
    float db = __builtin_bit_cast(float, __builtin_amdgcn_readlane(__builtin_bit_cast(int, dc), 16 * i + 8));
    u32x4 bm[2];
    step1(xa, xbf, ra, rb, da, db, 0, bm);
#pragma unroll
    for (int t = 0; t < 2; t++) {
      u32x4 bt = (g == i) ? tail[t] : (u32x4){0, 0, 0, 0};
#pragma unroll
      for (int T = 0; T < 3; T++) {
        f32x4 d = mfma16(tg[2 * T], bm[t], (f32x4){0, 0, 0, 0});
        d = mfma16(tg[2 * T + 1], bt, d);
        res[t][T][0][i] = pk(d[0], d[1]); res[t][T][1][i] = pk(d[2], d[3]);
      }
    }
  }
  waitl(); bar();
  STAMP(0,3);
#pragma unroll
  for (int t = 0; t < 2; t++)
#pragma unroll
    for (int T = 0; T < 3; T++)
#pragma unroll
      for (int h = 0; h < 2; h++)
        if (T < 2 || g < 2) *(LAS u32x4*)(sm + (16 * t + c + 32 * (8 * T + 2 * g + h)) * 128 + ((wv ^ (c & 7)) * 16)) = res[t][T][h];
  waitl(); bar();
#pragma unroll
  for (int i = 0; i < 10; i++) {
    int e = 512 * i + tid, kf = e >> 3, ch = e & 7;
    u32x4 v = ldsr(sm + kf * 128 + ((ch ^ (kf & 7)) * 16));
    st16(wsr(ws), OFF_X1 + kf * SX + w * 1024 + gq * 256 + (8 * o + ch) * 16, v);
  }
  STAMP(0,4);
}

extern "C" __global__ __launch_bounds__(768) __attribute__((amdgpu_waves_per_eu(3, 3))) void k1(const u16* x, const u16* wr, const u16* wi, const u16* w1, const u16* b1, const u16* w2, char* ws) {
  extern __shared__ __attribute__((aligned(16))) char smem[];
  LAS char* sm = (LAS char*)(size_t)0;
  if ((u32)(size_t)(LAS char*)smem != 0) return;
  const int tid = threadIdx.x, l = tid & 63, wv = __builtin_amdgcn_readfirstlane(tid >> 6);
  const int xcd = blockIdx.x & 7, idx = blockIdx.x >> 3;
  if (idx < 28) {
    const int w = xcd >> 1, g = idx / 7, ct = (xcd & 1) * 7 + idx % 7, gi = xcd * 28 + idx;
    const int rot0 = 0;
    if (wv < 8) {
      g1_compute(x, w1, ws, sm, tid, l, wv, w, g, ct, gi, idx % 7, rot0);
    } else if (wv < 10) g1_loader(x, w1, b1, sm, l, wv - 8, w, g, ct, rot0);
    else g1_side(wr, wi, w2, ws, sm, l, wv - 10, gi);
  } else if (wv < 8) stageA(x, ws, sm, tid, l, wv, xcd, idx);
}

DI u32x4 vneg_hi(u32x4 f) { return (u32x4){f.x, f.y, f.z ^ 0x80008000u, f.w ^ 0x80008000u}; }
DI u32x4 vswap(u32x4 f) { return (u32x4){f.z, f.w, f.x, f.y}; }
DI u32x4 vswapn(u32x4 f) { return (u32x4){f.z ^ 0x80008000u, f.w ^ 0x80008000u, f.x, f.y}; }
constexpr u32 CF_TAB = 118736, CF_EX = CF_TAB + 28672;
DI u32x4 pack8(f32x2 r0, f32x2 r1, f32x2 i0, f32x2 i1) { return (u32x4){pk(r0.x, r0.y), pk(r1.x, r1.y), pk(i0.x, i0.y), pk(i1.x, i1.y)}; }
DI u32x4 cmul(f32x4 dr, f32x4 di, const f32x2* c, const f32x2* s) {
  f32x2 a0 = {dr[0], dr[1]}, a1 = {dr[2], dr[3]}, b0 = {di[0], di[1]}, b1 = {di[2], di[3]};
  return pack8(a0 * c[0] - b0 * s[0], a1 * c[1] - b1 * s[1], b0 * c[0] + a0 * s[0], b1 * c[1] + a1 * s[1]);
}
#define PRE_ 8
constexpr int PRE = PRE_;
constexpr u32 K3_SY = 118720;
DI void lds_inc(u32 a) { asm volatile("s_mov_b64 exec, 1\n\tds_add_u32 %0, %1\n\ts_mov_b64 exec, -1" :: "v"(a), "v"(1u) : "memory"); }
DI u32 lds_inc_rtn(u32 a) {
  u32 v, r;
  asm volatile("s_mov_b64 exec, 1\n\tds_add_rtn_u32 %0, %2, %3\n\ts_mov_b64 exec, -1\n\ts_waitcnt lgkmcnt(0)\n\tv_readfirstlane_b32 %1, %0" : "=&v"(v), "=s"(r) : "v"(a), "v"(1u) : "memory");
  return r;
}
template <int SLEEP> DI void lds_wait(u32 a, u32 target) {
  u32 v, r, n;
  asm volatile("s_mov_b32 %2, 0x40000\n"
               "1:\n\tds_read_b32 %0, %3\n\ts_waitcnt lgkmcnt(0)\n\tv_readfirstlane_b32 %1, %0\n\ts_cmp_ge_u32 %1, %4\n\ts_cbranch_scc1 2f\n\t"
               ".if %5\n\ts_sleep %5\n\t.endif\n\t"
               "s_sub_u32 %2, %2, 1\n\ts_cbranch_scc0 1b\n"
               "2:" : "=&v"(v), "=&s"(r), "=&s"(n) : "v"(a), "s"(target), "n"(SLEEP) : "memory", "scc");
}
DI void lds_sync(u32 a, u32 target) { lds_inc(a); lds_wait<0>(a, target); }
template <int NPRE, class F, class G> DI void colfft(char* ws, LAS char* sm, int l, int grp, int w, int k, int nw, F&& after_loads, G&& ready) {
  const int c = l & 15, g = l >> 4, km = k ? 640 - k : 0;
  const rsrc_t wr_ = wsr(ws);
  const u32 lo = w * 1024 + l * 16;
  const u32x4 zp = __builtin_amdgcn_raw_buffer_load_b128(wr_, lo, OFF_X1 + k * SX, LDAUX);
  const u32x4 zm = __builtin_amdgcn_raw_buffer_load_b128(wr_, lo, OFF_X1 + km * SX, LDAUX);
  u32x4 wt[2];
  wt[0] = __builtin_amdgcn_raw_buffer_load_b128(wr_, l * 16, OFF_WT + k * SW + w * 2048, LDAUX);
  wt[1] = __builtin_amdgcn_raw_buffer_load_b128(wr_, l * 16 + 1024, OFF_WT + k * SW + w * 2048, LDAUX);
  {
    const int q = grp * 4 + w;
    if (nw == 8) {
      glds16(&dTabM1, q * 1024 + l * 16, CF_TAB + q * 1024);
      glds16(&dTabF64, q * 2048 + l * 16, CF_TAB + 12288 + q * 2048); glds16(&dTabF64, q * 2048 + 1024 + l * 16, CF_TAB + 13312 + q * 2048);
    } else {
      glds16(&dTabM1, q * 2048 + l * 16, CF_TAB + q * 2048); glds16(&dTabM1, q * 2048 + 1024 + l * 16, CF_TAB + 1024 + q * 2048);
      glds4c((const char*)&dTabF64 + q * 4096, CF_TAB + 12288 + q * 4096, l * 16);
    }
    if (q < 4) glds16(&dTabF32, q * 1024 + l * 16, CF_TAB + 8192 + q * 1024);
  }
  after_loads();
  constexpr float SC = 1.0f / (2.0f * 2048 * 640);
  f32x2 fc[2][2], fs[2][2], ic[2][2], is[2][2];
#pragma unroll
  for (int t = 0; t < 2; t++)
#pragma unroll
    for (int r = 0; r < 4; r++) {
      float a = (float)((16 * w + 4 * g + r) * (16 * t + c)) * (-1.0f / 2048), b = (float)((16 * w + c) * (16 * t + 4 * g + r)) * (1.0f / 2048);
      fc[t][r >> 1][r & 1] = __builtin_amdgcn_cosf(a); fs[t][r >> 1][r & 1] = __builtin_amdgcn_sinf(a);
      ic[t][r >> 1][r & 1] = __builtin_amdgcn_cosf(b) * SC; is[t][r >> 1][r & 1] = __builtin_amdgcn_sinf(b) * SC;
    }
#pragma unroll
  for (int t = 0; t < 2; t++)
#pragma unroll
    for (int r = 0; r < 2; r++) asm volatile("" : "+v"(fc[t][r]), "+v"(fs[t][r]), "+v"(ic[t][r]), "+v"(is[t][r]));
  waitv<NPRE>(); lds_sync(K3_SY, nw);
  ready();
  const f32x4 z4 = {0, 0, 0, 0};
  LAS char* e1 = sm + CF_EX + grp * 8192 + l * 16;
  LAS char* e2 = sm + CF_TAB + 12288 + grp * 8192 + l * 16;
  LAS char* tl = sm + CF_TAB + l * 16;
  u32x4 m1[8], f64[4], f32[4];
#pragma unroll
  for (int i = 0; i < 8; i++) m1[i] = ldsr(tl + i * 1024);
#pragma unroll
  for (int i = 0; i < 4; i++) f64[i] = ldsr(tl + 12288 + (w * 4 + i) * 1024);
  f32x4 dr[2], di[2];
#pragma unroll
  for (int t = 0; t < 2; t++) { dr[t] = mfma16(zp, m1[2 * t], z4); di[t] = mfma16(zp, m1[2 * t + 1], z4); }
#pragma unroll
  for (int t = 0; t < 2; t++) { dr[t] = mfma16(zm, m1[4 + 2 * t], dr[t]); di[t] = mfma16(zm, m1[5 + 2 * t], di[t]); }
#pragma unroll
  for (int t = 0; t < 2; t++) *(LAS u32x4*)(e1 + (t * 4 + w) * 1024) = cmul(dr[t], di[t], fc[t], fs[t]);
  lds_sync(K3_SY, 2 * nw);
  u32x4 b[8];
#pragma unroll
  for (int i = 0; i < 8; i++) b[i] = ldsr(e1 + i * 1024);
#pragma unroll
  for (int i = 0; i < 4; i++) {
    const u32x4 fa = vneg_hi(f64[i]), fb = vswap(f64[i]);
#pragma unroll
    for (int t = 0; t < 2; t++) { dr[t] = mfma16(fa, b[t * 4 + i], i ? dr[t] : z4); di[t] = mfma16(fb, b[t * 4 + i], i ? di[t] : z4); }
  }
#pragma unroll
  for (int t = 0; t < 2; t++) {
    const f32x2 wc[2] = {{blo(wt[t][0]), blo(wt[t][1])}, {blo(wt[t][2]), blo(wt[t][3])}}, wsn[2] = {{bhi(wt[t][0]), bhi(wt[t][1])}, {bhi(wt[t][2]), bhi(wt[t][3])}};
    *(LAS u32x4*)(e2 + (t * 4 + w) * 1024) = cmul(dr[t], di[t], wc, wsn);
  }
#pragma unroll
  for (int i = 0; i < 4; i++) f32[i] = ldsr(tl + 8192 + i * 1024);
  lds_sync(K3_SY + 4 + grp * 4, 4);
#pragma unroll
  for (int i = 0; i < 8; i++) b[i] = ldsr(e2 + i * 1024);
#pragma unroll
  for (int i = 0; i < 4; i++) {
    const u32x4 fb = vswapn(f64[i]);
#pragma unroll
    for (int t = 0; t < 2; t++) { dr[t] = mfma16(b[t * 4 + i], f64[i], i ? dr[t] : z4); di[t] = mfma16(b[t * 4 + i], fb, i ? di[t] : z4); }
  }
  const u32x4 u0 = cmul(dr[0], di[0], ic[0], is[0]), u1 = cmul(dr[1], di[1], ic[1], is[1]);
#pragma unroll
  for (int u = 0; u < 2; u++) { dr[u] = mfma16(u0, f32[2 * u], z4); di[u] = mfma16(u0, vswapn(f32[2 * u]), z4); }
#pragma unroll
  for (int u = 0; u < 2; u++) { dr[u] = mfma16(u1, f32[2 * u + 1], dr[u]); di[u] = mfma16(u1, vswapn(f32[2 * u + 1]), di[u]); }
  const u32 zo = OFF_ZT + (k == 0 ? 0 : k == 320 ? 1 : 2 * k) * SX;
  __builtin_amdgcn_raw_buffer_store_b128((u32x4){pk(dr[0][0], dr[0][1]), pk(dr[0][2], dr[0][3]), pk(dr[1][0], dr[1][1]), pk(dr[1][2], dr[1][3])}, wr_, lo + zo, 0, 16);
  if (k != 0 && k != 320)
    __builtin_amdgcn_raw_buffer_store_b128((u32x4){pk(di[0][0], di[0][1]), pk(di[0][2], di[0][3]), pk(di[1][0], di[1][1]), pk(di[1][2], di[1][3])}, wr_, lo + zo + SX, 0, 16);
  asm volatile("" ::: "memory");
}

constexpr u32 C_PD = 163836, C_ZF = 163832, C_ZS = 163828, C_ALL = 163824, C_ZW = 163820, C_FREE = 163816, C_TAIL = 163812, C_GO = 163808, C_PJ = 163804;
constexpr u32 K3_LDS = 163840, K3_D = 81920, K3_DA = 11264, K3_X = 138240;
DI void glds5(const void* sbase, u32 m0v, u32 v) {
  asm volatile("s_mov_b32 m0, %1\n\ts_nop 0\n\t" GL(2, -2048) GL(2, -1024) GL(2, 0) GL(2, 1024) GL(2, 2048) :: "s"(sbase), "s"(m0v), "v"(v) : "memory");
}
struct Rot { f32x2 p[4], q[4]; float t; };
DI void tab_init(Rot& r, int l, int d, int k0) {
  const int g = l >> 4, kd = ((k0 + 8 * (g >> 1) + 2 * (g & 1)) * d) % 640;
  const float a0 = (float)kd * (1.0f / 640), a1 = (float)d * (1.0f / 640), st = (float)((16 * d) % 640) * (1.0f / 640);
#pragma unroll
  for (int i = 0; i < 4; i++) {
    const float a = a0 + a1 * (4 * (i >> 1) + (i & 1)), b = a + st;
    r.p[i] = (f32x2){__builtin_amdgcn_cosf(a), __builtin_amdgcn_sinf(a)} * (f32x2){2.0f, -2.0f};
    r.q[i] = (f32x2){__builtin_amdgcn_cosf(b), __builtin_amdgcn_sinf(b)} * (f32x2){2.0f, -2.0f};
  }
  r.t = 2.0f * __builtin_amdgcn_cosf(st);
}
DI u32x4 tab_next(Rot& r) {
  u32x4 o;
#pragma unroll
  for (int i = 0; i < 4; i++) { o[i] = pk(r.p[i].x, r.p[i].y); const f32x2 n = r.q[i] * r.t - r.p[i]; r.p[i] = r.q[i]; r.q[i] = n; }
  return o;
}

extern "C" __global__ __launch_bounds__(768) __attribute__((amdgpu_waves_per_eu(3, 3))) void k3(const u16* x, const u16* b2, char* ws, u16* out) {
  extern __shared__ __attribute__((aligned(16))) char smem[];
  LAS char* sm = (LAS char*)(size_t)0;
  if ((u32)(size_t)(LAS char*)smem != 0) return;
  const int tid = threadIdx.x, l = tid & 63, wv = __builtin_amdgcn_readfirstlane(tid >> 6);
  const int bid = blockIdx.x, xcd = bid & 7, idx = bid >> 3, cg = xcd & 1;
  const int rb = 4 * (xcd >> 1) + (idx >> 3), ct = idx & 7, T = 2 * rb + cg;
  const rsrc_t wr_ = wsr(ws);
  gu32* cnt = (gu32*)(ws + OFF_CNT);
  TRDECL; TR(0);
  if (tid == 512) { *(LAS u32x4*)(sm + K3_SY) = (u32x4){0, 0, 0, 0}; *(LAS u32x4*)(sm + K3_LDS - 16) = (u32x4){0, 0, 0, 0}; *(LAS u32x4*)(sm + K3_LDS - 32) = (u32x4){0, 0, 0, 0}; *(LAS u32x4*)(sm + K3_LDS - 48) = (u32x4){0, 0, 0, 0}; waitl(); }
  auto srow = [&](int p) { return 64 * (8 * (T & 1) + (p >> 3) + 16 * ((p >> 2) & 1)) + 16 * (T >> 3) + 4 * ((T >> 1) & 3) + (p & 3); };
  const bool two = (bid & 3) == 0 || bid == 1;
  const int nw = two ? 8 : 4;
  auto zsignal = [&]() {
    if (lds_inc_rtn(C_ZS) == 7 && l == 0)
      __hip_atomic_fetch_add(cnt + (bid & 15) * 32, 1u, __ATOMIC_RELAXED, __HIP_MEMORY_SCOPE_AGENT);
  };
  auto pk4 = [](f32x4 v) { return (u32x2){pk(v[0], v[1]), pk(v[2], v[3])}; };
  LAS char* dl = sm + K3_D + (l & 15) * 176 + (l >> 4) * 8;
  const int sm2 = (wv >> 1) & 1, sk = wv & 1, sn = wv < 4 ? 0 : wv < 8 ? 4 : 2;
  u32x4 tb[2][10];
  auto fix0 = [&](int i) { if (sk == 0 && l < 16) tb[i][0].x = (l & 1) ? 0xbf803f80u : 0x3f803f80u; };
  const int sd = 80 * ct + 16 * sn + (l & 15);
  auto tabgen = [&](int i) {
    Rot rot; tab_init(rot, l, sd + 16 * i, 160 * sk);
#pragma unroll
    for (int j = 0; j < 10; j++) { tb[i][j] = tab_next(rot); asm volatile("" : "+v"(tb[i][j])); }
    fix0(i);
  };
  auto sphase = [&](int nn) {
    const int g = l >> 4, zr = 16 * (g >> 1) + 4 * (g & 1) + ((l & 15) >> 2);
    LAS char* za[2];
#pragma unroll
    for (int m = 0; m < 2; m++) za[m] = sm + sk * 40960 + zr * 128 + (((4 * sm2 + 2 * m + ((l & 3) >> 1)) ^ (((zr >> 1) & 3) * 2)) * 16) + 8 * (l & 1);
    f32x4 as[2][2] = {};
    u32x4 z[3][2];
    lds_wait<1>(C_ALL, 12);
#pragma unroll
    for (int j = 0; j < 3; j++)
#pragma unroll
      for (int m = 0; m < 2; m++) z[j][m] = trd2(za[m] + j * 4096, za[m] + j * 4096 + 1024);
#pragma unroll
    for (int j = 0; j < 10; j++) {
#pragma unroll
      for (int m = 0; m < 2; m++) {
#pragma unroll
        for (int i = 0; i < nn; i++) as[i][m] = mfma16(tb[i][j], z[j % 3][m], as[i][m]);
        SB;
        if (j < 7) z[j % 3][m] = trd2(za[m] + (j + 3) * 4096, za[m] + (j + 3) * 4096 + 1024);
        SB;
      }
    }
#pragma unroll
    for (int i = 0; i < nn; i++)
#pragma unroll
      for (int m = 0; m < 2; m++) *(LAS u32x2*)(dl + (3 + sk) * K3_DA + (2 * sm2 + m) * 2816 + (sn + i) * 32) = pk4(as[i][m]);
  };
  if (wv < 8) {
    const bool own = wv >= 4;
    const int mp = (wv >> 1) & 1, kp = wv & 1;
    u32x4 a[14][2], b[5];
    f32x4 ag[2][5];
#pragma unroll
    for (int n = 0; n < 5; n++) {
      u32x2 bb = *(const u32x2*)((const char*)b2 + ct * 160 + n * 32 + (l >> 4) * 8);
      if (wv < 4 || kp) bb = (u32x2){0, 0};
      ag[0][n] = ag[1][n] = (f32x4){blo(bb.x), bhi(bb.x), blo(bb.y), bhi(bb.y)};
    }
    Rot rot;
    const u32 ho = OFF_H + (((own ? T : T ^ 1) * 4 + 2 * mp) * 54 + 27 * cg + kp) * 1024;
    auto lda = [&](int j) {
#pragma unroll
      for (int m = 0; m < 2; m++) a[j][m] = __builtin_amdgcn_raw_buffer_load_b128(wr_, j == 13 && kp ? 0xf0000000u : l * 16 + (j & 1) * 2048, ho + m * (54 * 1024) + (j >> 1) * 4096, LDAUX);
    };
    const bool col = !own || two;
    auto pre = [&]() {
      bar();
#pragma unroll
      for (int j = 0; j < PRE / 2; j++) lda(j);
      asm volatile("" ::: "memory");
    };
    if (col) colfft<PRE>(ws, sm, l, wv >> 2, wv & 3, !own ? bid : bid == 1 ? 320 : 256 + (bid >> 2), nw, pre, [&]() { TR(1); });

    else pre();
    TR(2);
    waitv<0>(); zsignal();
    TR(3);
#pragma unroll
    for (int j = PRE / 2; j < AW; j++) lda(j);
    TR(4);
    bar();
    TR(5);
    LAS char* pb = sm + l * 16 + kp * 5120;
#pragma unroll
    for (int n = 0; n < 5; n++) b[n] = ldsr(pb + n * 1024);
#pragma unroll
    for (int j = 0; j < 14; j++) {
      if (j == 5 || j == 10) { pb += 51200; asm volatile("" : "+v"(pb)); }
      if (j == 12) pb -= kp * 5120;
      if (j == 10) lds_wait<0>(C_TAIL, 4);
      SB;
#pragma unroll
      for (int n = 0; n < 5; n++) {
        ag[0][n] = mfma16(b[n], a[j][0], ag[0][n]);
        ag[1][n] = mfma16(b[n], a[j][1], ag[1][n]);
        SB;
        if (j < 13) b[n] = ldsr(pb + (j + 1) * 10240 + n * 1024 - (j / 5) * 51200);
        if (n == 4 && j + AW < 14) lda(j + AW);
        if (n == 1 && j == 3) tab_init(rot, l, sd, 160 * sk);
        if (n == 1 && j >= 4) { tb[0][j - 4] = tab_next(rot); asm volatile("" : "+v"(tb[0][j - 4])); }
        SB;
      }
      if (j == 7) lds_inc(C_FREE);
    }
    TR(6);
    lds_inc(C_ALL);
    fix0(0);
    if (!own) {
      const u32 po = OFF_GP + bid * 20480 + wv * 5120;
#pragma unroll
      for (int i = 0; i < 5; i++) { const u32x2 u = pk4(ag[0][i]), v = pk4(ag[1][i]); __builtin_amdgcn_raw_buffer_store_b128((u32x4){u.x, u.y, v.x, v.y}, wr_, po + l * 16 + i * 1024, 0, 16); }
      asm volatile("" ::: "memory");
      tabgen(1);
      waitv<0>();
      if (lds_inc_rtn(C_PD) == 3 && l == 0) __hip_atomic_store(cnt + (16 + bid) * 32, 1u, __ATOMIC_RELAXED, __HIP_MEMORY_SCOPE_AGENT);
      TR(7);
      u32 v = __hip_atomic_load(cnt + (16 + (bid ^ 1)) * 32, __ATOMIC_RELAXED, __HIP_MEMORY_SCOPE_AGENT);
      SPIN(rfl(v) == 0, __builtin_amdgcn_s_sleep(4); v = __hip_atomic_load(cnt + (16 + (bid ^ 1)) * 32, __ATOMIC_RELAXED, __HIP_MEMORY_SCOPE_AGENT));
      asm volatile("" ::: "memory");
      TR(9);
      const u32 pi = OFF_GP + (bid ^ 1) * 20480 + mp * 10240 + kp * 3072;
      u32x4 p[2][3];
#pragma unroll
      for (int h = 0; h < 2; h++)
#pragma unroll
        for (int i = 0; i < 3; i++) p[h][i] = __builtin_amdgcn_raw_buffer_load_b128(wr_, l * 16 + i * 1024, pi + h * 5120, 16);
      sphase(2);
#pragma unroll
      for (int i = 0; i < 3; i++)
        if (i < 2 || !kp) {
          u32x4 q;
#pragma unroll
          for (int e = 0; e < 4; e++) q[e] = pk(blo(p[0][i][e]) + blo(p[1][i][e]), bhi(p[0][i][e]) + bhi(p[1][i][e]));
          LAS char* d = dl + 2 * K3_DA + mp * 5632 + (3 * kp + i) * 32;
          *(LAS u32x2*)d = (u32x2){q.x, q.y}; *(LAS u32x2*)(d + 2816) = (u32x2){q.z, q.w};
        }
    } else {
      lds_wait<1>(C_ALL, 12);
      TR(9);
#pragma unroll
      for (int m = 0; m < 2; m++)
#pragma unroll
        for (int n = 0; n < 5; n++) *(LAS u32x2*)(dl + kp * K3_DA + (2 * mp + m) * 2816 + n * 32) = pk4(ag[m][n]);
      sphase(1);
    }
  } else {
    const int hw = wv - 8;
    const char* bt = ws + OFF_BT + (ct * 54 + 27 * cg) * 5120;
    bar();
#pragma unroll 1
    for (int i = hw; i < 23; i += 4) glds5(bt + i * 5120 + 2048, i * 5120 + 2048, l * 16);
    TR(1);
    tabgen(0); tabgen(1);
    TR(2);
    waitv<0>();
    TR(4);
    bar();
    TR(5);
    glds5(bt + (23 + hw) * 5120 + 2048, (23 + hw) * 5120 + 2048, l * 16);
    waitv<0>(); lds_inc(C_TAIL);
#pragma unroll 1
    for (int i = hw; i < 10; i += 4) { const int e = 64 * i + l; glds16(x, srow(e / 10) * 1280 + ct * 160 + (e % 10) * 16, K3_X + i * 1024); }
    __builtin_amdgcn_s_setprio(3);
    lds_wait<4>(C_FREE, ZJ);
    TR(6);
    if (hw == 0) {
      u32 v = l < 16 ? __hip_atomic_load(cnt + l * 32, __ATOMIC_RELAXED, __HIP_MEMORY_SCOPE_AGENT) : 16u;
      SPIN(!__all(v == 16), __builtin_amdgcn_s_sleep(PSL); v = l < 16 ? __hip_atomic_load(cnt + l * 32, __ATOMIC_RELAXED, __HIP_MEMORY_SCOPE_AGENT) : 16u);
      lds_inc(C_ZF);
    } else lds_wait<2>(C_ZF, 1);
    TR(7);
    lds_wait<1>(C_FREE, 8);
    TR(3);
    {
      const char* zp = ws + OFF_ZT + T * 128 + hw * (160 * SX);
      const u32 zo = (l >> 3) * SX + (((l & 7) ^ (((l >> 4) & 3) * 2)) * 16);
      u32 vo[4];
#pragma unroll
      for (int i = 0; i < 4; i++) vo[i] = zo + i * (8 * SX - 1024);
#pragma unroll
      for (int i = 0; i < 5; i++)
        asm volatile("s_mov_b32 m0, %5\n\ts_nop 0\n\tglobal_load_lds_dwordx4 %0, %4 sc1\n\tglobal_load_lds_dwordx4 %1, %4 offset:1024 sc1\n\t"
                     "global_load_lds_dwordx4 %2, %4 offset:2048 sc1\n\tglobal_load_lds_dwordx4 %3, %4 offset:3072 sc1"
                     :: "v"(vo[0]), "v"(vo[1]), "v"(vo[2]), "v"(vo[3]), "s"(zp + i * (32 * SX)), "s"(hw * 20480 + i * 4096) : "memory");
      TR(8);
      waitv<0>();
      TR(9);
    }
    lds_inc(C_ALL);
    sphase(2);
  }
  TR(10);
  u32 ooff[2];
#pragma unroll
  for (int i = 0; i < 2; i++) { const int e = min(tid + 768 * i, 1279); ooff[i] = srow(e / 20) * 1280 + ct * 160 + (e % 20) * 8; asm volatile("" : "+v"(ooff[i])); }
  waitl(); bar();
#pragma unroll
  for (int i = 0; i < 2; i++) {
    const int e = tid + 768 * i;
    if (e < 1280) {
      LAS char* p = sm + K3_D + (e / 20) * 176 + (e % 20) * 8;
      u32x2 q[5];
#pragma unroll
      for (int k = 0; k < 5; k++) q[k] = *(LAS u32x2*)(p + k * K3_DA);
      const u32x2 xr = *(LAS u32x2*)(sm + K3_X + e * 8);
      u32 o[2];
#pragma unroll
      for (int j = 0; j < 2; j++) {
        const f32x2 v = (f32x2){blo(q[0][j]), bhi(q[0][j])} + (f32x2){blo(q[1][j]), bhi(q[1][j])} + (f32x2){blo(q[2][j]), bhi(q[2][j])};
        const f32x2 sv = (f32x2){blo(q[3][j]), bhi(q[3][j])} + (f32x2){blo(q[4][j]), bhi(q[4][j])};
        const f32x2 z = v * -1.4426950409f;
        const f32x2 d = (f32x2){__builtin_amdgcn_exp2f(z.x), __builtin_amdgcn_exp2f(z.y)} + 1.0f;
        const f32x2 r = sv * v * (f32x2){rcp(d.x), rcp(d.y)} + (f32x2){blo(xr[j]), bhi(xr[j])};
        o[j] = pk(r.x, r.y);
      }
      *(u32x2*)((char*)out + ooff[i]) = (u32x2){o[0], o[1]};
    }
  }
  TR(11);
  TREND;
}

extern "C" __global__ void kend() {}

extern "C" void kernel_launch(void* const* d_in, const int* in_sizes, int n_in, void* d_out, int out_size, void* d_ws, size_t ws_size, hipStream_t stream) {
  const u16* x = (const u16*)d_in[0];
  char* ws = (char*)d_ws;
  (void)hipFuncSetAttribute((const void*)k1, hipFuncAttributeMaxDynamicSharedMemorySize, K1_LDS);
  (void)hipFuncSetAttribute((const void*)k3, hipFuncAttributeMaxDynamicSharedMemorySize, K3_LDS);
  k1<<<256, 768, K1_LDS, stream>>>(x, (const u16*)d_in[1], (const u16*)d_in[2], (const u16*)d_in[4], (const u16*)d_in[5], (const u16*)d_in[6], ws);
  k3<<<256, 768, K3_LDS, stream>>>(x, (const u16*)d_in[7], ws, (u16*)d_out);
}
```
